```python
import torch

def _softmax(x, axis=-1):
    return torch.softmax(x, dim=axis)
def _gelu(x):
    return torch.nn.functional.gelu(x, approximate="tanh")
def _silu(x):
    return torch.nn.functional.silu(x)
def _sigmoid(x):
    return torch.sigmoid(x)
def _layernorm(x, axis=-1, eps=1e-5):
    m = x.mean(dim=axis, keepdim=True)
    v = x.var(dim=axis, keepdim=True, unbiased=False)
    return (x - m) / torch.sqrt(v + eps)
def _scatter_add(tgt, idx, src, axis=0):
    return tgt.clone().index_add_(axis, idx, src)
def _segment_sum(src, seg, n):
    out = torch.zeros((n,) + src.shape[1:], dtype=src.dtype)
    return out.index_add_(0, seg, src)
def _pad(x, pw):
    flat = tuple(v for pair in reversed(pw) for v in pair)
    return torch.nn.functional.pad(x, flat)

kernel_name = 'concat_heads__sliding_block_cached__gqa_block_cached__decode'

def setup_inputs():

    _w = (lambda a: (lambda t: t.to(torch.bfloat16) if t.is_floating_point() else t)(torch.as_tensor(a)))
    def _A_setup():
        B, Sq, D = 2, 1, 512
        Hq, Hkv, Sk, Dh = 32, 8, 8192, 128
        assert Hq % Hkv == 0, (Hq, Hkv)
        return {
            "x": _w((lambda *s: torch.randn(*s, dtype=torch.bfloat16) * 0.2)(B, Sq, D)),
            "k_cache": _w((lambda *s: torch.randn(*s, dtype=torch.bfloat16) * 3.2)(B, Hkv, Sk, Dh)),
            "v_cache": _w((lambda *s: torch.randn(*s, dtype=torch.bfloat16) * 1.6)(B, Hkv, Sk, Dh)),
            "wq": _w((lambda *s: torch.randn(*s, dtype=torch.bfloat16) * 3.2)(D, Hq * Dh) * (D ** -0.5)),
            "wo": _w((lambda *s: torch.randn(*s, dtype=torch.bfloat16) * 1.6)(Hq * Dh, D) * ((Hq * Dh) ** -0.5)),
        }
    _A_in = _A_setup()

    def _B_setup():
        B, Sq, D = 2, 1, 512
        Hq, Hkv, Sk, Dh = 32, 8, 8192, 128
        assert Hq % Hkv == 0, (Hq, Hkv)
        return {
            "x": _w((lambda *s: torch.randn(*s, dtype=torch.bfloat16) * 0.4)(B, Sq, D)),
            "k_cache": _w((lambda *s: torch.randn(*s, dtype=torch.bfloat16) * 3.2)(B, Hkv, Sk, Dh)),
            "v_cache": _w((lambda *s: torch.randn(*s, dtype=torch.bfloat16) * 0.8)(B, Hkv, Sk, Dh)),
            "wq": _w((lambda *s: torch.randn(*s, dtype=torch.bfloat16) * 3.2)(D, Hq * Dh) * (D ** -0.5)),
            "wo": _w((lambda *s: torch.randn(*s, dtype=torch.bfloat16) * 0.8)(Hq * Dh, D) * ((Hq * Dh) ** -0.5)),
        }
    _B_in = _B_setup()

    return {
        "A_x": _A_in["x"],
        "A_k_cache": _A_in["k_cache"],
        "A_v_cache": _A_in["v_cache"],
        "A_wq": _A_in["wq"],
        "A_wo": _A_in["wo"],
        "B_x": _B_in["x"],
        "B_k_cache": _B_in["k_cache"],
        "B_v_cache": _B_in["v_cache"],
        "B_wq": _B_in["wq"],
        "B_wo": _B_in["wo"],
    }

def reference(A_x, A_k_cache, A_v_cache, A_wq, A_wo, B_x, B_k_cache, B_v_cache, B_wq, B_wo):
    def _A_ref(x, k_cache, v_cache, wq, wo):
        B, Sq, D = x.shape
        Hq, Dh = 32, 128
        Hkv, Sk = k_cache.shape[1], k_cache.shape[2]
        W = min(4096, max(256, Sk // 4))
        rep = Hq // Hkv
        q = (lambda x, *d: x.permute(*d))((x @ wq).reshape(B, Sq, Hq, Dh), 0, 2, 1, 3)
        k = (lambda x, n, axis: torch.repeat_interleave(x, n, dim=axis))(k_cache[:, :, Sk - W:, :], rep, axis=1)
        v = (lambda x, n, axis: torch.repeat_interleave(x, n, dim=axis))(v_cache[:, :, Sk - W:, :], rep, axis=1)
        scores = torch.einsum("bhqd,bhkd->bhqk", q, k) * (Dh ** -0.5)
        p = _softmax(scores, axis=-1)
        o = torch.einsum("bhqk,bhkd->bhqd", p, v)
        return (lambda x, *d: x.permute(*d))(o, 0, 2, 1, 3).reshape(B, Sq, Hq * Dh) @ wo

    def _B_ref(x, k_cache, v_cache, wq, wo):
        B, Sq, D = x.shape
        Hq, Dh = 32, 128
        Hkv = k_cache.shape[1]
        rep = Hq // Hkv
        q = (lambda x, *d: x.permute(*d))((x @ wq).reshape(B, Sq, Hq, Dh), 0, 2, 1, 3)
        k = (lambda x, n, axis: torch.repeat_interleave(x, n, dim=axis))(k_cache, rep, axis=1)
        v = (lambda x, n, axis: torch.repeat_interleave(x, n, dim=axis))(v_cache, rep, axis=1)
        scores = torch.einsum("bhqd,bhkd->bhqk", q, k) * (Dh ** -0.5)
        p = _softmax(scores, axis=-1)
        o = torch.einsum("bhqk,bhkd->bhqd", p, v)
        return (lambda x, *d: x.permute(*d))(o, 0, 2, 1, 3).reshape(B, Sq, Hq * Dh) @ wo

    a = _A_ref(A_x, A_k_cache, A_v_cache, A_wq, A_wo)
    b = _B_ref(B_x, B_k_cache, B_v_cache, B_wq, B_wo)
    return torch.concatenate([a, b], axis=-1)
```

```cpp
#include <hip/hip_runtime.h>
#include <hip/hip_bf16.h>
#include <stdint.h>

typedef unsigned short u16;
using u32x4 = __attribute__((ext_vector_type(4))) unsigned;
__device__ __forceinline__ float bf2f(u16 v) { return __uint_as_float(((unsigned)v) << 16); }
__device__ __forceinline__ float lo(unsigned v) { return __uint_as_float(v << 16); }
__device__ __forceinline__ float hi(unsigned v) { return __uint_as_float(v & 0xffff0000u); }

constexpr int D = 512, HQ = 32, HKV = 8, SK = 8192, DH = 128, W = 2048, NQ = HQ * DH;
constexpr int NSPLIT = 16;

__global__ void k_q(const u16* xa, const u16* wqa, const u16* xb, const u16* wqb, float* q) {
  int br = blockIdx.y;
  const u16* x = br ? xb : xa; const u16* wq = br ? wqb : wqa;
  int col = blockIdx.x * 64 + (threadIdx.x & 63);
  int kq = threadIdx.x >> 6;
  float a0 = 0, a1 = 0;
  for (int k = kq * 128; k < kq * 128 + 128; ++k) {
    float w = bf2f(wq[(size_t)k * NQ + col]);
    a0 += bf2f(x[k]) * w; a1 += bf2f(x[D + k]) * w;
  }
  __shared__ float s[2][4][64];
  s[0][kq][threadIdx.x & 63] = a0; s[1][kq][threadIdx.x & 63] = a1;
  __syncthreads();
  if (threadIdx.x < 128) {
    int b = threadIdx.x >> 6, c = threadIdx.x & 63;
    float v = s[b][0][c] + s[b][1][c] + s[b][2][c] + s[b][3][c];
    v = __bfloat162float(__float2bfloat16(v));
    q[((size_t)br * 2 + b) * NQ + blockIdx.x * 64 + c] = v;
  }
}

__global__ void __launch_bounds__(256) k_attn(const u16* ka, const u16* va, const u16* kb, const u16* vb, const float* q, float* pm, float* po) {
  int s = blockIdx.x, bh = blockIdx.y, br = blockIdx.z;
  int b = bh >> 3, kvh = bh & 7;
  const u16* K = br ? kb : ka; const u16* V = br ? vb : va;
  int nk = br ? SK / NSPLIT : W / NSPLIT;
  int k0 = (br ? 0 : SK - W) + s * nk;
  K += ((size_t)bh * SK + k0) * DH; V += ((size_t)bh * SK + k0) * DH;
  __shared__ float qs[4][128];
  __shared__ float sc[4][512];
  __shared__ float red[4][2];
  for (int i = threadIdx.x; i < 512; i += 256) qs[i >> 7][i & 127] = q[((size_t)br * 2 + b) * NQ + kvh * 512 + i];
  __syncthreads();
  for (int j = threadIdx.x; j < nk; j += 256) {
    float a[4] = {0, 0, 0, 0};
    _Pragma("unroll 2") for (int d = 0; d < 128; d += 8) {
      u32x4 v = *(const u32x4*)(K + (size_t)j * DH + d);
      for (int e = 0; e < 4; ++e) for (int g = 0; g < 4; ++g) a[g] += lo(v[e]) * qs[g][d + 2 * e] + hi(v[e]) * qs[g][d + 2 * e + 1];
    }
    for (int g = 0; g < 4; ++g) sc[g][j] = a[g] * 0.088388347648318440f;
  }
  __syncthreads();
  int g = threadIdx.x >> 6, lane = threadIdx.x & 63;
  float m = -INFINITY;
  for (int j = lane; j < nk; j += 64) m = fmaxf(m, sc[g][j]);
  for (int o = 32; o; o >>= 1) m = fmaxf(m, __shfl_xor(m, o));
  float l = 0;
  for (int j = lane; j < nk; j += 64) { float p = __expf(sc[g][j] - m); sc[g][j] = p; l += p; }
  for (int o = 32; o; o >>= 1) l += __shfl_xor(l, o);
  __syncthreads();
  float o0 = 0, o1 = 0;
  for (int j = 0; j < nk; ++j) { unsigned v = *(const unsigned*)(V + (size_t)j * DH + 2 * lane); float p = sc[g][j]; o0 += p * lo(v); o1 += p * hi(v); }
  size_t slot = (((size_t)br * 2 + b) * HQ + kvh * 4 + g) * NSPLIT + s;
  po[slot * DH + 2 * lane] = o0; po[slot * DH + 2 * lane + 1] = o1;
  if (lane == 0) { pm[slot * 2] = m; pm[slot * 2 + 1] = l; }
}

__global__ void k_o(const float* pm, const float* po, const u16* woa, const u16* wob, float* part) {
  int h = blockIdx.x, br = blockIdx.y;
  const u16* wo = br ? wob : woa;
  __shared__ float os[2][128];
  int b = threadIdx.x >> 7, d = threadIdx.x & 127;
  size_t slot = (((size_t)br * 2 + b) * HQ + h) * NSPLIT;
  float M = -INFINITY;
  for (int s = 0; s < NSPLIT; ++s) M = fmaxf(M, pm[(slot + s) * 2]);
  float L = 0, o = 0;
  for (int s = 0; s < NSPLIT; ++s) { float w = __expf(pm[(slot + s) * 2] - M); L += w * pm[(slot + s) * 2 + 1]; o += w * po[(slot + s) * DH + d]; }
  os[b][d] = __bfloat162float(__float2bfloat16(o / L));
  __syncthreads();
  float a00 = 0, a01 = 0, a10 = 0, a11 = 0;
  int c = threadIdx.x * 2;
  for (int r = 0; r < 128; ++r) {
    unsigned w = *(const unsigned*)(wo + (size_t)(h * 128 + r) * D + c);
    a00 += os[0][r] * lo(w); a01 += os[0][r] * hi(w); a10 += os[1][r] * lo(w); a11 += os[1][r] * hi(w);
  }
  float* p = part + ((size_t)br * 32 + h) * 1024;
  p[c] = a00; p[c + 1] = a01; p[512 + c] = a10; p[512 + c + 1] = a11;
}

__global__ void k_fin(const float* part, __hip_bfloat16* out) {
  int i = blockIdx.x * 256 + threadIdx.x;
  int br = i >> 10, b = (i >> 9) & 1, c = i & 511;
  float a = 0;
  for (int h = 0; h < 32; ++h) a += part[((size_t)br * 32 + h) * 1024 + b * 512 + c];
  out[b * 1024 + br * 512 + c] = __float2bfloat16(a);
}

extern "C" void kernel_launch(void* const* d_in, const int* in_sizes, int n_in, void* d_out, int out_size, void* d_ws, size_t ws_size, hipStream_t stream) {
  const u16* xa = (const u16*)d_in[0]; const u16* ka = (const u16*)d_in[1]; const u16* va = (const u16*)d_in[2]; const u16* wqa = (const u16*)d_in[3]; const u16* woa = (const u16*)d_in[4];
  const u16* xb = (const u16*)d_in[5]; const u16* kb = (const u16*)d_in[6]; const u16* vb = (const u16*)d_in[7]; const u16* wqb = (const u16*)d_in[8]; const u16* wob = (const u16*)d_in[9];
  float* q = (float*)d_ws; float* pm = q + 4 * NQ; float* po = pm + 4 * HQ * NSPLIT * 2; float* part = po + 4 * HQ * NSPLIT * DH;
  k_q<<<dim3(64, 2), 256, 0, stream>>>(xa, wqa, xb, wqb, q);
  k_attn<<<dim3(NSPLIT, 16, 2), 256, 0, stream>>>(ka, va, kb, vb, q, pm, po);
  k_o<<<dim3(32, 2), 256, 0, stream>>>(pm, po, woa, wob, part);
  k_fin<<<8, 256, 0, stream>>>(part, (__hip_bfloat16*)d_out);
}
```

```cpp
#define THR 2700
#define STAMP 1
#include <hip/hip_runtime.h>
#include <stdint.h>

#define AUX_KV 2
#define AUX_W 2
#define NK1 12
#ifndef NV1
#define NV1 12
#endif

typedef unsigned short u16;
typedef unsigned u32;
typedef unsigned long long u64;
using u32x2 = __attribute__((ext_vector_type(2))) unsigned;
using u32x4 = __attribute__((ext_vector_type(4))) unsigned;
using f32x2 = __attribute__((ext_vector_type(2))) float;
using f32x4 = __attribute__((ext_vector_type(4))) float;
using bf16x8 = __attribute__((ext_vector_type(8))) __bf16;
using bf16x2 = __attribute__((ext_vector_type(2))) __bf16;
using s16x4 = __attribute__((ext_vector_type(4))) short;
typedef __amdgpu_buffer_rsrc_t rsrc_t;
#define GAS __attribute__((address_space(1)))

constexpr int SK = 8192, WIN = 2048;
constexpr u32 WS_Q = 4096, WS_ML = WS_Q + 65536, WS_O = 1u << 20, WS_P = WS_O + (2u << 20), WS_END = WS_P + (2u << 20);
constexpr u32 WS_STAMP = 8u << 20;
constexpr int SC1 = 16;
constexpr u32 SPIN_CAP = 1u << 16;
constexpr float SCALE = 0.088388347648318440f, L2E = 1.4426950408889634f;

constexpr int L_RED = 0, RED_U = 288, RED_B = 2 * RED_U, RED_W = 2 * RED_B;
constexpr int L_QP = L_RED + 8 * RED_W;
constexpr int L_QS = L_QP + 512;
constexpr int L_MX = L_QS + 512;
constexpr int L_LS = L_MX + 64;
constexpr int L_OB = L_LS + 64;
constexpr int L_END = L_OB + 32;

__device__ __forceinline__ rsrc_t mk(const void* p, u32 bytes) { return __builtin_amdgcn_make_buffer_rsrc((void*)p, 0, bytes, 0x00020000); }
template <int AUX> __device__ __forceinline__ u32x4 ld16(rsrc_t r, u32 vo, u32 so) { return __builtin_amdgcn_raw_buffer_load_b128(r, vo, so, AUX); }
__device__ __forceinline__ void st16(rsrc_t r, u32 vo, u32 so, u32x4 v) { __builtin_amdgcn_raw_buffer_store_b128(v, r, vo, so, SC1); }
__device__ __forceinline__ float blo(u32 v) { return __uint_as_float(v << 16); }
__device__ __forceinline__ float bhi(u32 v) { return __uint_as_float(v & 0xffff0000u); }
__device__ __forceinline__ u32 pk(float lo, float hi) { bf16x2 r = {(__bf16)lo, (__bf16)hi}; return __builtin_bit_cast(u32, r); }
template <int CTRL> __device__ __forceinline__ float dpp(float x) { return __builtin_bit_cast(float, __builtin_amdgcn_mov_dpp(__builtin_bit_cast(int, x), CTRL, 0xf, 0xf, true)); }
constexpr int ROR1 = 0x121, ROR2 = 0x122, ROR4 = 0x124, ROR8 = 0x128, SHL4 = 0x104, SHR4 = 0x114;
__device__ __forceinline__ float tr32(float a, float b) { auto s = __builtin_amdgcn_permlane32_swap(__float_as_uint(a), __float_as_uint(b), false, false); return __uint_as_float(s[0]) + __uint_as_float(s[1]); }
__device__ __forceinline__ float tr16(float a, float b) { auto s = __builtin_amdgcn_permlane16_swap(__float_as_uint(a), __float_as_uint(b), false, false); return __uint_as_float(s[0]) + __uint_as_float(s[1]); }
__device__ __forceinline__ float xmax(float x) {
  auto s = __builtin_amdgcn_permlane16_swap(__float_as_uint(x), __float_as_uint(x), false, false);
  x = fmaxf(__uint_as_float(s[0]), __uint_as_float(s[1]));
  auto t = __builtin_amdgcn_permlane32_swap(__float_as_uint(x), __float_as_uint(x), false, false);
  return fmaxf(__uint_as_float(t[0]), __uint_as_float(t[1]));
}
__device__ __forceinline__ float xsum(float x) { x = tr16(x, x); return tr32(x, x); }
__device__ __forceinline__ float rowsum(float x) { x += dpp<ROR8>(x); x += dpp<ROR4>(x); x += dpp<ROR2>(x); x += dpp<ROR1>(x); return x; }
__device__ __forceinline__ float rowmax(float x) { x = fmaxf(x, dpp<ROR8>(x)); x = fmaxf(x, dpp<ROR4>(x)); x = fmaxf(x, dpp<ROR2>(x)); x = fmaxf(x, dpp<ROR1>(x)); return x; }
__device__ __forceinline__ float dot2(u32 a, u32 b, float c) { return __builtin_amdgcn_fdot2_f32_bf16(__builtin_bit_cast(bf16x2, a), __builtin_bit_cast(bf16x2, b), c, false); }
#define BAR() asm volatile("s_waitcnt lgkmcnt(0)\n\ts_barrier" ::: "memory")
#define PIN() __builtin_amdgcn_sched_barrier(0)
#define FENCE(off) asm volatile("" : "+v"(off) :: "memory")
__device__ __forceinline__ bool allok(bool ok) { return __builtin_amdgcn_ballot_w64(!ok) == 0ull; }
#define ST(i) do { PIN(); stamp[i] = __builtin_amdgcn_s_memrealtime(); PIN(); } while (0)

__global__ void __launch_bounds__(512, 2) fused_decode(const u16* __restrict__ xa, const u16* __restrict__ ka, const u16* __restrict__ va, const u16* __restrict__ wqa, const u16* __restrict__ woa,
                                                       const u16* __restrict__ xb, const u16* __restrict__ kb, const u16* __restrict__ vb, const u16* __restrict__ wqb, const u16* __restrict__ wob,
                                                       u16* __restrict__ out, unsigned char* ws) {
  __shared__ __attribute__((aligned(16))) float lds[L_END];
  u32* ldsu = reinterpret_cast<u32*>(lds);
  u64 stamp[12];
  ST(0);
  const int tid = threadIdx.x, lane = tid & 63, wave = __builtin_amdgcn_readfirstlane(tid >> 6), wg = blockIdx.x;
  const int hb = wg >> 7, loc = wg & 127;
  const int bh = wg >> 4, sp = wg & 15, b = bh >> 3, kvh = bh & 7;
  const rsrc_t rws = mk(ws, WS_END);
  const u64 ep = __hip_atomic_load((GAS u64*)ws, __ATOMIC_RELAXED, __HIP_MEMORY_SCOPE_AGENT);

  const int cs = (loc & ~15) | ((loc & 7) << 1) | ((loc >> 3) & 1);
  const u16* x = hb ? xb : xa;
  const u32x2 x0 = *(const u32x2*)(x + 64 * wave + 4 * (lane >> 2)), x1 = *(const u32x2*)(x + 512 + 64 * wave + 4 * (lane >> 2));
  u32x4 w[4];
  { const rsrc_t r = mk((hb ? wqb : wqa) + (size_t)64 * wave * 4096 + cs * 32, 63 * 8192 + 64);
    const u32 vo = (lane >> 2) * 32768 + (lane & 3) * 16;
#pragma unroll
    for (int t = 0; t < 4; ++t) w[t] = ld16<AUX_W>(r, vo, t * 8192); }
  PIN();
  const rsrc_t rka = mk(ka + ((size_t)bh * SK + (SK - WIN) + sp * 128 + wave * 16) * 128, 4096);
  const rsrc_t rkb = mk(kb + ((size_t)bh * SK + sp * 512 + wave * 64) * 128, 16384);
  const rsrc_t rva = mk(va + ((size_t)bh * SK + (SK - WIN) + sp * 128 + wave * 16) * 128, 4096);
  const rsrc_t rvb = mk(vb + ((size_t)bh * SK + sp * 512 + wave * 64) * 128, 16384);
  const u32 kvo = (4 * (lane & 3) + ((lane >> 2) & 3)) * 256 + (lane >> 4) * 16, vvo = lane * 16;
  u32x4 K[20], V[20];
#define LDK(i) K[i] = ((i) < 4 ? ld16<AUX_KV>(rka, kvo + ((i) & 3) * 64, 0) : ld16<AUX_KV>(rkb, kvo + ((i) & 3) * 64, (((i) >> 2) - 1) * 4096))
#define LDV(i) V[i] = ((i) < 4 ? ld16<AUX_KV>(rva, vvo + ((i) & 3) * 1024, 0) : ld16<AUX_KV>(rvb, vvo + ((i) & 3) * 1024, (((i) >> 2) - 1) * 4096))
#pragma unroll
  for (int i = 0; i < NK1; ++i) LDK(i);
  PIN();
  ST(1);
  const u32 tagA = __builtin_amdgcn_readfirstlane((u32)ep) + 1u, tagB = __builtin_amdgcn_readfirstlane((u32)(ep >> 32)) + 1u;
  {
    float a[2][8];
#pragma unroll
    for (int e = 0; e < 8; ++e) { a[0][e] = 0.f; a[1][e] = 0.f; }
#pragma unroll
    for (int t = 0; t < 4; ++t) {
      const float f0 = (t & 1) ? bhi(x0[t >> 1]) : blo(x0[t >> 1]), f1 = (t & 1) ? bhi(x1[t >> 1]) : blo(x1[t >> 1]);
#pragma unroll
      for (int e = 0; e < 4; ++e) { const float l = blo(w[t][e]), h = bhi(w[t][e]);
        a[0][2 * e] = fmaf(f0, l, a[0][2 * e]); a[0][2 * e + 1] = fmaf(f0, h, a[0][2 * e + 1]);
        a[1][2 * e] = fmaf(f1, l, a[1][2 * e]); a[1][2 * e + 1] = fmaf(f1, h, a[1][2 * e + 1]); } }
    float y[8], z[4], u[2];
#pragma unroll
    for (int e = 0; e < 8; ++e) y[e] = tr32(a[0][e], a[1][e]);
#pragma unroll
    for (int e = 0; e < 4; ++e) z[e] = tr16(y[e], y[e + 4]);
#pragma unroll
    for (int e = 0; e < 2; ++e) { const float t1 = z[e] + dpp<ROR8>(z[e]), t2 = z[e + 2] + dpp<ROR8>(z[e + 2]); u[e] = (lane & 8) ? t2 : t1; }
    const float t1 = u[0] + dpp<SHL4>(u[0]), t2 = u[1] + dpp<SHR4>(u[1]);
    const float qv = (lane & 4) ? t2 : t1;
    lds[L_QP + wave * 64 + (lane & 32) + 8 * (lane & 3) + ((lane >> 2) & 4) + ((lane >> 2) & 2) + ((lane >> 2) & 1)] = qv;
  }
  BAR();
  if (wave == 0) {
    const int l16 = lane & 15;
    f32x4 s = *(const f32x4*)&lds[L_QP + 4 * l16];
#pragma unroll
    for (int ww = 1; ww < 8; ++ww) s += *(const f32x4*)&lds[L_QP + ww * 64 + 4 * l16];
    const u32x4 g = {pk(s[0], s[1]), tagA, pk(s[2], s[3]), tagB};
    if (lane < 16) st16(rws, WS_Q + ((hb * 2 + (l16 >> 3)) * 1024 + cs * 8 + (l16 & 7)) * 16, 0, g);
  }
  PIN();
  ST(2);
#pragma unroll
  for (int i = NK1; i < 20; ++i) LDK(i);
  PIN();
  const int qi = wave * 32 + (lane & 31);
  u32 qoff = WS_Q + (((qi >> 7) * 2 + b) * 1024 + kvh * 128 + (qi & 127)) * 16;
  u32x4 qg = ld16<SC1>(rws, qoff, 0);
  PIN();
#pragma unroll
  for (int i = 0; i < NV1; ++i) LDV(i);
  PIN();
  ST(3);
  if (!allok(qg[1] == tagA && qg[3] == tagB)) {
    u32 spins = 0;
    do { FENCE(qoff); qg = ld16<SC1>(rws, qoff, 0); } while (!allok(qg[1] == tagA && qg[3] == tagB) && ++spins < SPIN_CAP);
  }
  *(u32x2*)&ldsu[L_QS + 2 * qi] = u32x2{qg[0], qg[2]};
  BAR();
  ST(4);
  float s[5][4];
#pragma unroll
  for (int br = 0; br < 2; ++br) {
    u32x4 qf[4];
#pragma unroll
    for (int j = 0; j < 4; ++j) qf[j] = *(const u32x4*)&ldsu[L_QS + br * 256 + ((lane >> 2) & 3) * 64 + j * 16 + (lane >> 4) * 4];
#pragma unroll
    for (int t = br; t < (br ? 5 : 1); ++t) {
      f32x4 c = {0.f, 0.f, 0.f, 0.f};
#pragma unroll
      for (int j = 0; j < 4; ++j) c = __builtin_amdgcn_mfma_f32_16x16x32_bf16(__builtin_bit_cast(bf16x8, K[4 * t + j]), __builtin_bit_cast(bf16x8, qf[j]), c, 0, 0, 0);
#pragma unroll
      for (int r = 0; r < 4; r += 2) { const u32 p1 = pk(c[r], c[r + 1]); const u32 p2 = pk(blo(p1) * SCALE, bhi(p1) * SCALE); s[t][r] = blo(p2); s[t][r + 1] = bhi(p2); }
    }
  }
  PIN();
#pragma unroll
  for (int i = NV1; i < 20; ++i) LDV(i);
  { const rsrc_t r = mk((hb ? wob : woa) + (size_t)32 * loc * 512 + 64 * wave, 31 * 1024 + 128);
    const u32 vo = (lane >> 3) * 4096 + (lane & 7) * 16;
#pragma unroll
    for (int t = 0; t < 4; ++t) w[t] = ld16<AUX_W>(r, vo + t * 1024, 0); }
  PIN();
  float m2[2];
  {
    float ma = fmaxf(fmaxf(s[0][0], s[0][1]), fmaxf(s[0][2], s[0][3])), mb = -INFINITY;
#pragma unroll
    for (int t = 1; t < 5; ++t) mb = fmaxf(mb, fmaxf(fmaxf(s[t][0], s[t][1]), fmaxf(s[t][2], s[t][3])));
    ma = xmax(ma); mb = xmax(mb);
    if ((lane & 0x33) == 0) { lds[L_MX + (lane >> 2) * 8 + wave] = ma; lds[L_MX + 32 + (lane >> 2) * 8 + wave] = mb; }
  }
  BAR();
#pragma unroll
  for (int br = 0; br < 2; ++br) {
    const f32x4 a = *(const f32x4*)&lds[L_MX + br * 32 + ((lane >> 2) & 3) * 8], c = *(const f32x4*)&lds[L_MX + br * 32 + ((lane >> 2) & 3) * 8 + 4];
    m2[br] = fmaxf(fmaxf(fmaxf(a[0], a[1]), fmaxf(a[2], a[3])), fmaxf(fmaxf(c[0], c[1]), fmaxf(c[2], c[3]))) * L2E;
  }
  u32 P[20];
  {
    float la = 0.f, lb = 0.f;
#pragma unroll
    for (int t = 0; t < 5; ++t)
#pragma unroll
      for (int r = 0; r < 4; ++r) { const float p = __builtin_amdgcn_exp2f(fmaf(s[t][r], L2E, -m2[t ? 1 : 0])); if (t) lb += p; else la += p; P[4 * t + r] = pk(p, p); }
    la = xsum(la); lb = xsum(lb);
    if ((lane & 0x33) == 0) { lds[L_LS + (lane >> 2) * 8 + wave] = la; lds[L_LS + 32 + (lane >> 2) * 8 + wave] = lb; }
  }
  BAR();
  if (wave == 7) {
    const int i8 = lane & 7;
    const f32x4 a = *(const f32x4*)&lds[L_MX + i8 * 8], c = *(const f32x4*)&lds[L_MX + i8 * 8 + 4];
    const f32x4 d = *(const f32x4*)&lds[L_LS + i8 * 8], e = *(const f32x4*)&lds[L_LS + i8 * 8 + 4];
    const float mm = fmaxf(fmaxf(fmaxf(a[0], a[1]), fmaxf(a[2], a[3])), fmaxf(fmaxf(c[0], c[1]), fmaxf(c[2], c[3]))) * L2E;
    const float ll = ((d[0] + d[1]) + (d[2] + d[3])) + ((e[0] + e[1]) + (e[2] + e[3]));
    const u32x4 g = {__float_as_uint(mm), tagA, __float_as_uint(ll), tagB};
    if (lane < 8) st16(rws, WS_ML + (wg * 8 + i8) * 16, 0, g);
  }
  PIN();
  ST(5);
  const int ep4 = lane & 3;
  const u32 mk0 = ep4 == 0 ? 0x0000ffffu : ep4 == 1 ? 0xffff0000u : 0u, mk1 = ep4 == 2 ? 0x0000ffffu : ep4 == 3 ? 0xffff0000u : 0u;
  f32x4 o[2][2];
#pragma unroll
  for (int br = 0; br < 2; ++br) { o[br][0] = f32x4{0.f, 0.f, 0.f, 0.f}; o[br][1] = f32x4{0.f, 0.f, 0.f, 0.f}; }
#pragma unroll
  for (int i = 0; i < 20; ++i) {
    const int br = i >= 4;
    const s16x4 a = __builtin_bit_cast(s16x4, u32x2{P[i] & mk0, P[i] & mk1});
    o[br][0] = __builtin_amdgcn_mfma_f32_16x16x16bf16_1k(a, __builtin_bit_cast(s16x4, u32x2{V[i][0], V[i][1]}), o[br][0], 0, 0, 0);
    o[br][1] = __builtin_amdgcn_mfma_f32_16x16x16bf16_1k(a, __builtin_bit_cast(s16x4, u32x2{V[i][2], V[i][3]}), o[br][1], 0, 0, 0);
    if (i == 3) { *(f32x4*)&lds[L_RED + wave * RED_W + 4 * lane] = o[0][0]; *(f32x4*)&lds[L_RED + wave * RED_W + RED_U + 4 * lane] = o[0][1]; }
  }
  *(f32x4*)&lds[L_RED + wave * RED_W + RED_B + 4 * lane] = o[1][0];
  *(f32x4*)&lds[L_RED + wave * RED_W + RED_B + RED_U + 4 * lane] = o[1][1];
  ST(6);
  BAR();
  {
    const int base = L_RED + (wave >> 2) * RED_B + ((lane >> 1) & 1) * RED_U + (16 * (wave & 3) + (lane >> 2)) * 4 + 2 * (lane & 1);
    f32x2 t[8];
#pragma unroll
    for (int ww = 0; ww < 8; ++ww) t[ww] = *(const f32x2*)&lds[base + ww * RED_W];
    const f32x2 sum = ((t[0] + t[1]) + (t[2] + t[3])) + ((t[4] + t[5]) + (t[6] + t[7]));
    const u32x4 g = {__float_as_uint(sum[0]), tagA, __float_as_uint(sum[1]), tagB};
    st16(rws, WS_O + lane * 16, ((((wave >> 2) * 2 + b) * 32 + kvh * 4 + (wave & 3)) * 16 + sp) * 1024, g);
  }
  PIN();
  ST(7);
  {
    const int ob = wave >> 2, head = loc >> 2, pl = 4 * (wave & 3) + (lane >> 4);
    u32 moff = WS_ML + (((ob * 8 + (head >> 2)) * 16 + (lane & 15)) * 8 + hb * 4 + (head & 3)) * 16;
    u32 ooff = WS_O + (((hb * 2 + ob) * 32 + head) * 16 + (lane & 15)) * 1024 + (16 * (loc & 3) + pl) * 16;
    u32x4 mg = ld16<SC1>(rws, moff, 0);
    u32x4 og = ld16<SC1>(rws, ooff, 0);
    PIN();
    u32 wp[2][8];
#pragma unroll
    for (int e = 0; e < 4; ++e) {
      wp[0][2 * e] = __builtin_amdgcn_perm(w[1][e], w[0][e], 0x05040100u); wp[0][2 * e + 1] = __builtin_amdgcn_perm(w[1][e], w[0][e], 0x07060302u);
      wp[1][2 * e] = __builtin_amdgcn_perm(w[3][e], w[2][e], 0x05040100u); wp[1][2 * e + 1] = __builtin_amdgcn_perm(w[3][e], w[2][e], 0x07060302u); }
    PIN();
    if (!allok(mg[1] == tagA && mg[3] == tagB)) {
      u32 spins = 0;
      do { FENCE(moff); mg = ld16<SC1>(rws, moff, 0); } while (!allok(mg[1] == tagA && mg[3] == tagB) && ++spins < SPIN_CAP);
    }
    const float ms = __uint_as_float(mg[0]), M = rowmax(ms), ex = __builtin_amdgcn_exp2f(ms - M);
    const float wgt = ex / rowsum(ex * __uint_as_float(mg[2]));
    PIN();
    if (!allok(og[1] == tagA && og[3] == tagB)) {
      u32 spins = 0;
      do { FENCE(ooff); og = ld16<SC1>(rws, ooff, 0); } while (!allok(og[1] == tagA && og[3] == tagB) && ++spins < SPIN_CAP);
    }
    ST(8);
    const float o0 = rowsum(wgt * __uint_as_float(og[0])), o1 = rowsum(wgt * __uint_as_float(og[2]));
    if ((lane & 15) == 0) ldsu[L_OB + (pl >> 1) * 4 + ob * 2 + (pl & 1)] = pk(o0, o1);
    BAR();
    const u32x4 ov = *(const u32x4*)&ldsu[L_OB + (lane >> 3) * 4];
    float y[8], z[4], u[2];
#pragma unroll
    for (int e = 0; e < 8; ++e) y[e] = tr32(dot2(wp[1][e], ov[1], dot2(wp[0][e], ov[0], 0.f)), dot2(wp[1][e], ov[3], dot2(wp[0][e], ov[2], 0.f)));
#pragma unroll
    for (int e = 0; e < 4; ++e) z[e] = tr16(y[e], y[e + 4]);
#pragma unroll
    for (int e = 0; e < 2; ++e) { const float t1 = z[e] + dpp<ROR8>(z[e]), t2 = z[e + 2] + dpp<ROR8>(z[e + 2]); u[e] = (lane & 8) ? t2 : t1; }
    const u32x4 g = {__float_as_uint(u[0]), tagA, __float_as_uint(u[1]), tagB};
    st16(rws, WS_P + ((lane >> 5) * 256 + 4 * (lane & 7) + ((lane >> 3) & 2) + ((lane >> 3) & 1)) * 16, (wg * 512 + wave * 32) * 16, g);
  }
  PIN();
  ST(9);
  if (wave == 0) {
    const int fb = (wg >> 6) & 1, cg = wg & 63;
    u32 vo = WS_P + hb * (1u << 20) + fb * 4096 + cg * 64 + (lane >> 2) * 8192 + (lane & 3) * 16;
    u32x4 g[8]; bool ok; u32 spins = 0;
    do {
      FENCE(vo); ok = true;
#pragma unroll
      for (int u = 0; u < 8; ++u) g[u] = ld16<SC1>(rws, vo, u * 131072);
#pragma unroll
      for (int u = 0; u < 8; ++u) ok = ok && g[u][1] == tagA && g[u][3] == tagB;
    } while (!allok(ok) && ++spins < SPIN_CAP);
    ST(10);
    f32x2 t[8];
#pragma unroll
    for (int u = 0; u < 8; ++u) t[u] = f32x2{__uint_as_float(g[u][0]), __uint_as_float(g[u][2])};
    const f32x2 sum = ((t[0] + t[1]) + (t[2] + t[3])) + ((t[4] + t[5]) + (t[6] + t[7]));
    float a0 = sum[0], a1 = sum[1];
    a0 += dpp<ROR4>(a0); a1 += dpp<ROR4>(a1); a0 += dpp<ROR8>(a0); a1 += dpp<ROR8>(a1);
    a0 = xsum(a0); a1 = xsum(a1);
    if (lane < 4) *(u32*)(out + fb * 1024 + hb * 512 + cg * 8 + 2 * lane) = pk(a0, a1);
    if (wg == 0 && lane == 0) __hip_atomic_store((GAS u64*)ws, ((u64)tagB << 32) | tagA, __ATOMIC_RELAXED, __HIP_MEMORY_SCOPE_AGENT);
    ST(11);
    if (lane == 0 && wg == 0 && (u32)(stamp[11] - stamp[0]) > THR) {
      __builtin_amdgcn_s_waitcnt(0);
      const int id[8] = {1,2,3,4,6,8,10,11};
#pragma unroll
      for (int i = 0; i < 4; ++i) *(u32*)(out + 2 * i) = pk((float)(u32)(stamp[id[2*i]] - stamp[0]) * 0.01f, (float)(u32)(stamp[id[2*i+1]] - stamp[0]) * 0.01f); }
  }
}

extern "C" void kernel_launch(void* const* d_in, const int* in_sizes, int n_in, void* d_out, int out_size, void* d_ws, size_t ws_size, hipStream_t stream) {
  fused_decode<<<256, 512, 0, stream>>>((const u16*)d_in[0], (const u16*)d_in[1], (const u16*)d_in[2], (const u16*)d_in[3], (const u16*)d_in[4],
                                        (const u16*)d_in[5], (const u16*)d_in[6], (const u16*)d_in[7], (const u16*)d_in[8], (const u16*)d_in[9],
                                        (u16*)d_out, (unsigned char*)d_ws);
}
```

```cpp
#include <hip/hip_runtime.h>
#include <stdint.h>

#define AUX_KV 2
#define AUX_W 2
#define NK1 12
#ifndef NV1
#define NV1 12
#endif
#define STAMP 0

typedef unsigned short u16;
typedef unsigned u32;
typedef unsigned long long u64;
using u32x2 = __attribute__((ext_vector_type(2))) unsigned;
using u32x4 = __attribute__((ext_vector_type(4))) unsigned;
using f32x2 = __attribute__((ext_vector_type(2))) float;
using f32x4 = __attribute__((ext_vector_type(4))) float;
using bf16x8 = __attribute__((ext_vector_type(8))) __bf16;
using bf16x2 = __attribute__((ext_vector_type(2))) __bf16;
using s16x4 = __attribute__((ext_vector_type(4))) short;
typedef __amdgpu_buffer_rsrc_t rsrc_t;
#define GAS __attribute__((address_space(1)))

constexpr int SK = 8192, WIN = 2048;
constexpr u32 WS_Q = 4096, WS_ML = WS_Q + 65536, WS_O = 1u << 20, WS_P = WS_O + (2u << 20), WS_END = WS_P + (2u << 20);
constexpr u32 WS_STAMP = 8u << 20;
constexpr int SC1 = 16;
constexpr u32 SPIN_CAP = 1u << 16;
constexpr float SCALE = 0.088388347648318440f, L2E = 1.4426950408889634f;

constexpr int L_RED = 0, RED_U = 288, RED_B = 2 * RED_U, RED_W = 2 * RED_B;
constexpr int L_QP = L_RED + 8 * RED_W;
constexpr int L_QS = L_QP + 512;
constexpr int L_MX = L_QS + 512;
constexpr int L_LS = L_MX + 64;
constexpr int L_OB = L_LS + 64;
constexpr int L_END = L_OB + 32;

__device__ __forceinline__ rsrc_t mk(const void* p, u32 bytes) { return __builtin_amdgcn_make_buffer_rsrc((void*)p, 0, bytes, 0x00020000); }
template <int AUX> __device__ __forceinline__ u32x4 ld16(rsrc_t r, u32 vo, u32 so) { return __builtin_amdgcn_raw_buffer_load_b128(r, vo, so, AUX); }
__device__ __forceinline__ void st16(rsrc_t r, u32 vo, u32 so, u32x4 v) { __builtin_amdgcn_raw_buffer_store_b128(v, r, vo, so, SC1); }
__device__ __forceinline__ float blo(u32 v) { return __uint_as_float(v << 16); }
__device__ __forceinline__ float bhi(u32 v) { return __uint_as_float(v & 0xffff0000u); }
__device__ __forceinline__ u32 pk(float lo, float hi) { bf16x2 r = {(__bf16)lo, (__bf16)hi}; return __builtin_bit_cast(u32, r); }
template <int CTRL> __device__ __forceinline__ float dpp(float x) { return __builtin_bit_cast(float, __builtin_amdgcn_mov_dpp(__builtin_bit_cast(int, x), CTRL, 0xf, 0xf, true)); }
constexpr int ROR1 = 0x121, ROR2 = 0x122, ROR4 = 0x124, ROR8 = 0x128, SHL4 = 0x104, SHR4 = 0x114;
__device__ __forceinline__ float tr32(float a, float b) { auto s = __builtin_amdgcn_permlane32_swap(__float_as_uint(a), __float_as_uint(b), false, false); return __uint_as_float(s[0]) + __uint_as_float(s[1]); }
__device__ __forceinline__ float tr16(float a, float b) { auto s = __builtin_amdgcn_permlane16_swap(__float_as_uint(a), __float_as_uint(b), false, false); return __uint_as_float(s[0]) + __uint_as_float(s[1]); }
__device__ __forceinline__ float xmax(float x) {
  auto s = __builtin_amdgcn_permlane16_swap(__float_as_uint(x), __float_as_uint(x), false, false);
  x = fmaxf(__uint_as_float(s[0]), __uint_as_float(s[1]));
  auto t = __builtin_amdgcn_permlane32_swap(__float_as_uint(x), __float_as_uint(x), false, false);
  return fmaxf(__uint_as_float(t[0]), __uint_as_float(t[1]));
}
__device__ __forceinline__ float xsum(float x) { x = tr16(x, x); return tr32(x, x); }
__device__ __forceinline__ float rowsum(float x) { x += dpp<ROR8>(x); x += dpp<ROR4>(x); x += dpp<ROR2>(x); x += dpp<ROR1>(x); return x; }
__device__ __forceinline__ float rowmax(float x) { x = fmaxf(x, dpp<ROR8>(x)); x = fmaxf(x, dpp<ROR4>(x)); x = fmaxf(x, dpp<ROR2>(x)); x = fmaxf(x, dpp<ROR1>(x)); return x; }
__device__ __forceinline__ float dot2(u32 a, u32 b, float c) { return __builtin_amdgcn_fdot2_f32_bf16(__builtin_bit_cast(bf16x2, a), __builtin_bit_cast(bf16x2, b), c, false); }
#define BAR() asm volatile("s_waitcnt lgkmcnt(0)\n\ts_barrier" ::: "memory")
#define PIN() __builtin_amdgcn_sched_barrier(0)
#define FENCE(off) asm volatile("" : "+v"(off) :: "memory")
__device__ __forceinline__ bool allok(bool ok) { return __builtin_amdgcn_ballot_w64(!ok) == 0ull; }
#define ST(i)

__global__ void __launch_bounds__(512, 2) fused_decode(const u16* __restrict__ xa, const u16* __restrict__ ka, const u16* __restrict__ va, const u16* __restrict__ wqa, const u16* __restrict__ woa,
                                                       const u16* __restrict__ xb, const u16* __restrict__ kb, const u16* __restrict__ vb, const u16* __restrict__ wqb, const u16* __restrict__ wob,
                                                       u16* __restrict__ out, unsigned char* ws) {
  __shared__ __attribute__((aligned(16))) float lds[L_END];
  u32* ldsu = reinterpret_cast<u32*>(lds);
  ST(0);
  const int tid = threadIdx.x, lane = tid & 63, wave = __builtin_amdgcn_readfirstlane(tid >> 6), wg = blockIdx.x;
  const int hb = wg >> 7, loc = wg & 127;
  const int bh = wg >> 4, sp = wg & 15, b = bh >> 3, kvh = bh & 7;
  const rsrc_t rws = mk(ws, WS_END);
  const u64 ep = __hip_atomic_load((GAS u64*)ws, __ATOMIC_RELAXED, __HIP_MEMORY_SCOPE_AGENT);

  const int cs = (loc & ~15) | ((loc & 7) << 1) | ((loc >> 3) & 1);
  const u16* x = hb ? xb : xa;
  const u32x2 x0 = *(const u32x2*)(x + 64 * wave + 4 * (lane >> 2)), x1 = *(const u32x2*)(x + 512 + 64 * wave + 4 * (lane >> 2));
  u32x4 w[4];
  { const rsrc_t r = mk((hb ? wqb : wqa) + (size_t)64 * wave * 4096 + cs * 32, 63 * 8192 + 64);
    const u32 vo = (lane >> 2) * 32768 + (lane & 3) * 16;
#pragma unroll
    for (int t = 0; t < 4; ++t) w[t] = ld16<AUX_W>(r, vo, t * 8192); }
  PIN();
  const rsrc_t rka = mk(ka + ((size_t)bh * SK + (SK - WIN) + sp * 128 + wave * 16) * 128, 4096);
  const rsrc_t rkb = mk(kb + ((size_t)bh * SK + sp * 512 + wave * 64) * 128, 16384);
  const rsrc_t rva = mk(va + ((size_t)bh * SK + (SK - WIN) + sp * 128 + wave * 16) * 128, 4096);
  const rsrc_t rvb = mk(vb + ((size_t)bh * SK + sp * 512 + wave * 64) * 128, 16384);
  const u32 kvo = (4 * (lane & 3) + ((lane >> 2) & 3)) * 256 + (lane >> 4) * 16, vvo = lane * 16;
  u32x4 K[20], V[20];
#define LDK(i) K[i] = ((i) < 4 ? ld16<AUX_KV>(rka, kvo + ((i) & 3) * 64, 0) : ld16<AUX_KV>(rkb, kvo + ((i) & 3) * 64, (((i) >> 2) - 1) * 4096))
#define LDV(i) V[i] = ((i) < 4 ? ld16<AUX_KV>(rva, vvo + ((i) & 3) * 1024, 0) : ld16<AUX_KV>(rvb, vvo + ((i) & 3) * 1024, (((i) >> 2) - 1) * 4096))
#pragma unroll
  for (int i = 0; i < NK1; ++i) LDK(i);
  PIN();
  ST(1);
  const u32 tagA = __builtin_amdgcn_readfirstlane((u32)ep) + 1u, tagB = __builtin_amdgcn_readfirstlane((u32)(ep >> 32)) + 1u;
  {
    float a[2][8];
#pragma unroll
    for (int e = 0; e < 8; ++e) { a[0][e] = 0.f; a[1][e] = 0.f; }
#pragma unroll
    for (int t = 0; t < 4; ++t) {
      const float f0 = (t & 1) ? bhi(x0[t >> 1]) : blo(x0[t >> 1]), f1 = (t & 1) ? bhi(x1[t >> 1]) : blo(x1[t >> 1]);
#pragma unroll
      for (int e = 0; e < 4; ++e) { const float l = blo(w[t][e]), h = bhi(w[t][e]);
        a[0][2 * e] = fmaf(f0, l, a[0][2 * e]); a[0][2 * e + 1] = fmaf(f0, h, a[0][2 * e + 1]);
        a[1][2 * e] = fmaf(f1, l, a[1][2 * e]); a[1][2 * e + 1] = fmaf(f1, h, a[1][2 * e + 1]); } }
    float y[8], z[4], u[2];
#pragma unroll
    for (int e = 0; e < 8; ++e) y[e] = tr32(a[0][e], a[1][e]);
#pragma unroll
    for (int e = 0; e < 4; ++e) z[e] = tr16(y[e], y[e + 4]);
#pragma unroll
    for (int e = 0; e < 2; ++e) { const float t1 = z[e] + dpp<ROR8>(z[e]), t2 = z[e + 2] + dpp<ROR8>(z[e + 2]); u[e] = (lane & 8) ? t2 : t1; }
    const float t1 = u[0] + dpp<SHL4>(u[0]), t2 = u[1] + dpp<SHR4>(u[1]);
    const float qv = (lane & 4) ? t2 : t1;
    lds[L_QP + wave * 64 + (lane & 32) + 8 * (lane & 3) + ((lane >> 2) & 4) + ((lane >> 2) & 2) + ((lane >> 2) & 1)] = qv;
  }
  BAR();
  if (wave == 0) {
    const int l16 = lane & 15;
    f32x4 s = *(const f32x4*)&lds[L_QP + 4 * l16];
#pragma unroll
    for (int ww = 1; ww < 8; ++ww) s += *(const f32x4*)&lds[L_QP + ww * 64 + 4 * l16];
    const u32x4 g = {pk(s[0], s[1]), tagA, pk(s[2], s[3]), tagB};
    if (lane < 16) st16(rws, WS_Q + ((hb * 2 + (l16 >> 3)) * 1024 + cs * 8 + (l16 & 7)) * 16, 0, g);
  }
  PIN();
  ST(2);
#pragma unroll
  for (int i = NK1; i < 20; ++i) LDK(i);
  PIN();
  const int qi = wave * 32 + (lane & 31);
  u32 qoff = WS_Q + (((qi >> 7) * 2 + b) * 1024 + kvh * 128 + (qi & 127)) * 16;
  u32x4 qg = ld16<SC1>(rws, qoff, 0);
  PIN();
#pragma unroll
  for (int i = 0; i < NV1; ++i) LDV(i);
  PIN();
  ST(3);
  if (!allok(qg[1] == tagA && qg[3] == tagB)) {
    u32 spins = 0;
    do { FENCE(qoff); qg = ld16<SC1>(rws, qoff, 0); } while (!allok(qg[1] == tagA && qg[3] == tagB) && ++spins < SPIN_CAP);
  }
  *(u32x2*)&ldsu[L_QS + 2 * qi] = u32x2{qg[0], qg[2]};
  BAR();
  ST(4);
  float s[5][4];
#pragma unroll
  for (int br = 0; br < 2; ++br) {
    u32x4 qf[4];
#pragma unroll
    for (int j = 0; j < 4; ++j) qf[j] = *(const u32x4*)&ldsu[L_QS + br * 256 + ((lane >> 2) & 3) * 64 + j * 16 + (lane >> 4) * 4];
#pragma unroll
    for (int t = br; t < (br ? 5 : 1); ++t) {
      f32x4 c = {0.f, 0.f, 0.f, 0.f};
#pragma unroll
      for (int j = 0; j < 4; ++j) c = __builtin_amdgcn_mfma_f32_16x16x32_bf16(__builtin_bit_cast(bf16x8, K[4 * t + j]), __builtin_bit_cast(bf16x8, qf[j]), c, 0, 0, 0);
#pragma unroll
      for (int r = 0; r < 4; r += 2) { const u32 p1 = pk(c[r], c[r + 1]); const u32 p2 = pk(blo(p1) * SCALE, bhi(p1) * SCALE); s[t][r] = blo(p2); s[t][r + 1] = bhi(p2); }
    }
  }
  PIN();
#pragma unroll
  for (int i = NV1; i < 20; ++i) LDV(i);
  { const rsrc_t r = mk((hb ? wob : woa) + (size_t)32 * loc * 512 + 64 * wave, 31 * 1024 + 128);
    const u32 vo = (lane >> 3) * 4096 + (lane & 7) * 16;
#pragma unroll
    for (int t = 0; t < 4; ++t) w[t] = ld16<AUX_W>(r, vo + t * 1024, 0); }
  PIN();
  float m2[2];
  {
    float ma = fmaxf(fmaxf(s[0][0], s[0][1]), fmaxf(s[0][2], s[0][3])), mb = -INFINITY;
#pragma unroll
    for (int t = 1; t < 5; ++t) mb = fmaxf(mb, fmaxf(fmaxf(s[t][0], s[t][1]), fmaxf(s[t][2], s[t][3])));
    ma = xmax(ma); mb = xmax(mb);
    if ((lane & 0x33) == 0) { lds[L_MX + (lane >> 2) * 8 + wave] = ma; lds[L_MX + 32 + (lane >> 2) * 8 + wave] = mb; }
  }
  BAR();
#pragma unroll
  for (int br = 0; br < 2; ++br) {
    const f32x4 a = *(const f32x4*)&lds[L_MX + br * 32 + ((lane >> 2) & 3) * 8], c = *(const f32x4*)&lds[L_MX + br * 32 + ((lane >> 2) & 3) * 8 + 4];
    m2[br] = fmaxf(fmaxf(fmaxf(a[0], a[1]), fmaxf(a[2], a[3])), fmaxf(fmaxf(c[0], c[1]), fmaxf(c[2], c[3]))) * L2E;
  }
  u32 P[20];
  {
    float la = 0.f, lb = 0.f;
#pragma unroll
    for (int t = 0; t < 5; ++t)
#pragma unroll
      for (int r = 0; r < 4; ++r) { const float p = __builtin_amdgcn_exp2f(fmaf(s[t][r], L2E, -m2[t ? 1 : 0])); if (t) lb += p; else la += p; P[4 * t + r] = pk(p, p); }
    la = xsum(la); lb = xsum(lb);
    if ((lane & 0x33) == 0) { lds[L_LS + (lane >> 2) * 8 + wave] = la; lds[L_LS + 32 + (lane >> 2) * 8 + wave] = lb; }
  }
  BAR();
  if (wave == 7) {
    const int i8 = lane & 7;
    const f32x4 a = *(const f32x4*)&lds[L_MX + i8 * 8], c = *(const f32x4*)&lds[L_MX + i8 * 8 + 4];
    const f32x4 d = *(const f32x4*)&lds[L_LS + i8 * 8], e = *(const f32x4*)&lds[L_LS + i8 * 8 + 4];
    const float mm = fmaxf(fmaxf(fmaxf(a[0], a[1]), fmaxf(a[2], a[3])), fmaxf(fmaxf(c[0], c[1]), fmaxf(c[2], c[3]))) * L2E;
    const float ll = ((d[0] + d[1]) + (d[2] + d[3])) + ((e[0] + e[1]) + (e[2] + e[3]));
    const u32x4 g = {__float_as_uint(mm), tagA, __float_as_uint(ll), tagB};
    if (lane < 8) st16(rws, WS_ML + (wg * 8 + i8) * 16, 0, g);
  }
  PIN();
  ST(5);
  const int ep4 = lane & 3;
  const u32 mk0 = ep4 == 0 ? 0x0000ffffu : ep4 == 1 ? 0xffff0000u : 0u, mk1 = ep4 == 2 ? 0x0000ffffu : ep4 == 3 ? 0xffff0000u : 0u;
  f32x4 o[2][2];
#pragma unroll
  for (int br = 0; br < 2; ++br) { o[br][0] = f32x4{0.f, 0.f, 0.f, 0.f}; o[br][1] = f32x4{0.f, 0.f, 0.f, 0.f}; }
#pragma unroll
  for (int i = 0; i < 20; ++i) {
    const int br = i >= 4;
    const s16x4 a = __builtin_bit_cast(s16x4, u32x2{P[i] & mk0, P[i] & mk1});
    o[br][0] = __builtin_amdgcn_mfma_f32_16x16x16bf16_1k(a, __builtin_bit_cast(s16x4, u32x2{V[i][0], V[i][1]}), o[br][0], 0, 0, 0);
    o[br][1] = __builtin_amdgcn_mfma_f32_16x16x16bf16_1k(a, __builtin_bit_cast(s16x4, u32x2{V[i][2], V[i][3]}), o[br][1], 0, 0, 0);
    if (i == 3) { *(f32x4*)&lds[L_RED + wave * RED_W + 4 * lane] = o[0][0]; *(f32x4*)&lds[L_RED + wave * RED_W + RED_U + 4 * lane] = o[0][1]; }
  }
  *(f32x4*)&lds[L_RED + wave * RED_W + RED_B + 4 * lane] = o[1][0];
  *(f32x4*)&lds[L_RED + wave * RED_W + RED_B + RED_U + 4 * lane] = o[1][1];
  ST(6);
  BAR();
  {
    const int base = L_RED + (wave >> 2) * RED_B + ((lane >> 1) & 1) * RED_U + (16 * (wave & 3) + (lane >> 2)) * 4 + 2 * (lane & 1);
    f32x2 t[8];
#pragma unroll
    for (int ww = 0; ww < 8; ++ww) t[ww] = *(const f32x2*)&lds[base + ww * RED_W];
    const f32x2 sum = ((t[0] + t[1]) + (t[2] + t[3])) + ((t[4] + t[5]) + (t[6] + t[7]));
    const u32x4 g = {__float_as_uint(sum[0]), tagA, __float_as_uint(sum[1]), tagB};
    st16(rws, WS_O + lane * 16, ((((wave >> 2) * 2 + b) * 32 + kvh * 4 + (wave & 3)) * 16 + sp) * 1024, g);
  }
  PIN();
  ST(7);
  {
    const int ob = wave >> 2, head = loc >> 2, pl = 4 * (wave & 3) + (lane >> 4);
    u32 moff = WS_ML + (((ob * 8 + (head >> 2)) * 16 + (lane & 15)) * 8 + hb * 4 + (head & 3)) * 16;
    u32 ooff = WS_O + (((hb * 2 + ob) * 32 + head) * 16 + (lane & 15)) * 1024 + (16 * (loc & 3) + pl) * 16;
    u32x4 mg = ld16<SC1>(rws, moff, 0);
    u32x4 og = ld16<SC1>(rws, ooff, 0);
    PIN();
    u32 wp[2][8];
#pragma unroll
    for (int e = 0; e < 4; ++e) {
      wp[0][2 * e] = __builtin_amdgcn_perm(w[1][e], w[0][e], 0x05040100u); wp[0][2 * e + 1] = __builtin_amdgcn_perm(w[1][e], w[0][e], 0x07060302u);
      wp[1][2 * e] = __builtin_amdgcn_perm(w[3][e], w[2][e], 0x05040100u); wp[1][2 * e + 1] = __builtin_amdgcn_perm(w[3][e], w[2][e], 0x07060302u); }
    PIN();
    if (!allok(mg[1] == tagA && mg[3] == tagB)) {
      u32 spins = 0;
      do { FENCE(moff); mg = ld16<SC1>(rws, moff, 0); } while (!allok(mg[1] == tagA && mg[3] == tagB) && ++spins < SPIN_CAP);
    }
    const float ms = __uint_as_float(mg[0]), M = rowmax(ms), ex = __builtin_amdgcn_exp2f(ms - M);
    const float wgt = ex / rowsum(ex * __uint_as_float(mg[2]));
    PIN();
    if (!allok(og[1] == tagA && og[3] == tagB)) {
      u32 spins = 0;
      do { FENCE(ooff); og = ld16<SC1>(rws, ooff, 0); } while (!allok(og[1] == tagA && og[3] == tagB) && ++spins < SPIN_CAP);
    }
    ST(8);
    const float o0 = rowsum(wgt * __uint_as_float(og[0])), o1 = rowsum(wgt * __uint_as_float(og[2]));
    if ((lane & 15) == 0) ldsu[L_OB + (pl >> 1) * 4 + ob * 2 + (pl & 1)] = pk(o0, o1);
    BAR();
    const u32x4 ov = *(const u32x4*)&ldsu[L_OB + (lane >> 3) * 4];
    float y[8], z[4], u[2];
#pragma unroll
    for (int e = 0; e < 8; ++e) y[e] = tr32(dot2(wp[1][e], ov[1], dot2(wp[0][e], ov[0], 0.f)), dot2(wp[1][e], ov[3], dot2(wp[0][e], ov[2], 0.f)));
#pragma unroll
    for (int e = 0; e < 4; ++e) z[e] = tr16(y[e], y[e + 4]);
#pragma unroll
    for (int e = 0; e < 2; ++e) { const float t1 = z[e] + dpp<ROR8>(z[e]), t2 = z[e + 2] + dpp<ROR8>(z[e + 2]); u[e] = (lane & 8) ? t2 : t1; }
    const u32x4 g = {__float_as_uint(u[0]), tagA, __float_as_uint(u[1]), tagB};
    st16(rws, WS_P + ((lane >> 5) * 256 + 4 * (lane & 7) + ((lane >> 3) & 2) + ((lane >> 3) & 1)) * 16, (wg * 512 + wave * 32) * 16, g);
  }
  PIN();
  ST(9);
  if (wave == 0) {
    const int fb = (wg >> 6) & 1, cg = wg & 63;
    u32 vo = WS_P + hb * (1u << 20) + fb * 4096 + cg * 64 + (lane >> 2) * 8192 + (lane & 3) * 16;
    u32x4 g[8]; bool ok; u32 spins = 0;
    do {
      FENCE(vo); ok = true;
#pragma unroll
      for (int u = 0; u < 8; ++u) g[u] = ld16<SC1>(rws, vo, u * 131072);
#pragma unroll
      for (int u = 0; u < 8; ++u) ok = ok && g[u][1] == tagA && g[u][3] == tagB;
    } while (!allok(ok) && ++spins < SPIN_CAP);
    ST(10);
    f32x2 t[8];
#pragma unroll
    for (int u = 0; u < 8; ++u) t[u] = f32x2{__uint_as_float(g[u][0]), __uint_as_float(g[u][2])};
    const f32x2 sum = ((t[0] + t[1]) + (t[2] + t[3])) + ((t[4] + t[5]) + (t[6] + t[7]));
    float a0 = sum[0], a1 = sum[1];
    a0 += dpp<ROR4>(a0); a1 += dpp<ROR4>(a1); a0 += dpp<ROR8>(a0); a1 += dpp<ROR8>(a1);
    a0 = xsum(a0); a1 = xsum(a1);
    if (lane < 4) *(u32*)(out + fb * 1024 + hb * 512 + cg * 8 + 2 * lane) = pk(a0, a1);
    if (wg == 0 && lane == 0) __hip_atomic_store((GAS u64*)ws, ((u64)tagB << 32) | tagA, __ATOMIC_RELAXED, __HIP_MEMORY_SCOPE_AGENT);
  }
}

extern "C" void kernel_launch(void* const* d_in, const int* in_sizes, int n_in, void* d_out, int out_size, void* d_ws, size_t ws_size, hipStream_t stream) {
  fused_decode<<<256, 512, 0, stream>>>((const u16*)d_in[0], (const u16*)d_in[1], (const u16*)d_in[2], (const u16*)d_in[3], (const u16*)d_in[4],
                                        (const u16*)d_in[5], (const u16*)d_in[6], (const u16*)d_in[7], (const u16*)d_in[8], (const u16*)d_in[9],
                                        (u16*)d_out, (unsigned char*)d_ws);
}
```

```cpp
#include <hip/hip_runtime.h>
#include <stdint.h>

typedef unsigned short u16;
typedef unsigned u32;
typedef unsigned long long u64;
using u32x2 = __attribute__((ext_vector_type(2))) unsigned;
using u32x4 = __attribute__((ext_vector_type(4))) unsigned;
using f32x2 = __attribute__((ext_vector_type(2))) float;
using f32x4 = __attribute__((ext_vector_type(4))) float;
using bf16x8 = __attribute__((ext_vector_type(8))) __bf16;
using bf16x2 = __attribute__((ext_vector_type(2))) __bf16;
using s16x4 = __attribute__((ext_vector_type(4))) short;
typedef __amdgpu_buffer_rsrc_t rsrc_t;

constexpr int SK = 8192, WIN = 2048;
constexpr u32 WS_VT = 2048, WS_Q = 4096, WS_ML = 4096 + 65536, WS_PR = 1u << 20, WS_O = 2u << 20, WS_P = 4u << 20, WS_END = 6u << 20;
constexpr u32 OOB = 0x80000000u;
constexpr int SC1 = 16, NT = 2;
constexpr u32 SPIN_CAP = 1u << 16;
constexpr float SCALE = 0.088388347648318440f, L2E = 1.4426950408889634f;
constexpr int DEP = 3, NPRE = 2, NSO = 6, QP = 19, QC = 22, QW = 23, ISYNC = 10, HOIST = 12, MP = 18;
#define SLOWN 24
#define SLOWS 8
#define STAG 5

constexpr int L_RED = 0, RED_U = 288, RED_B = 2 * RED_U, RED_W = 2 * RED_B;
constexpr int L_QP = L_RED + 8 * RED_W;
constexpr int L_QS = L_QP + 512;
constexpr int L_MX = L_QS + 512;
constexpr int L_LS = L_MX + 64;
constexpr int L_OB = L_LS + 64;
constexpr int L_TG = L_OB + 32;
constexpr int L_CN = L_TG + 4;
constexpr int L_FS = L_TG + 8;
constexpr int L_END = L_FS + 128;
constexpr u32 TM = 0xfffff000u;

#define MAPS "y0czpqxba13d2"
constexpr int kpos(char c, bool win) { int p = 0; for (int i = 0; MAPS[i] != c; ++i) if (!win || (MAPS[i] != 'c' && MAPS[i] != 'd')) ++p; return p; }
template <char C0, int N, bool W> __device__ __forceinline__ u32 dep(u32 x) { if constexpr (N == 0) return 0u; else { constexpr int p = kpos(C0, W) + 8; return ((x & 1u) << p) | dep<C0 + 1, N - 1, W>(x >> 1); } }
__device__ __forceinline__ rsrc_t mk(const void* p, u32 bytes) { return __builtin_amdgcn_make_buffer_rsrc((void*)p, 0, bytes, 0x00020000); }
template <int AUX> __device__ __forceinline__ u32x4 ld16(rsrc_t r, u32 vo, u32 so) { return __builtin_amdgcn_raw_buffer_load_b128(r, vo, so, AUX); }
__device__ __forceinline__ void st16(rsrc_t r, u32 vo, u32 so, u32x4 v) { __builtin_amdgcn_raw_buffer_store_b128(v, r, vo, so, SC1); }
template <int AUX> __device__ __forceinline__ u32x2 ld8(rsrc_t r, u32 vo, u32 so) { return __builtin_amdgcn_raw_buffer_load_b64(r, vo, so, AUX); }
__device__ __forceinline__ void st8(rsrc_t r, u32 vo, u32 so, u32x2 v) { __builtin_amdgcn_raw_buffer_store_b64(v, r, vo, so, SC1); }
__device__ __forceinline__ u32 tagw(float v, u32 t) { return ((__float_as_uint(v) + 0x800u) & TM) | (t & ~TM); }
__device__ __forceinline__ float untag(u32 v) { return __uint_as_float(v & TM); }
__device__ __forceinline__ float blo(u32 v) { return __uint_as_float(v << 16); }
__device__ __forceinline__ float bhi(u32 v) { return __uint_as_float(v & 0xffff0000u); }
__device__ __forceinline__ u32 pk(float lo, float hi) { bf16x2 r = {(__bf16)lo, (__bf16)hi}; return __builtin_bit_cast(u32, r); }
template <int CTRL> __device__ __forceinline__ float dpp(float x) { return __builtin_bit_cast(float, __builtin_amdgcn_mov_dpp(__builtin_bit_cast(int, x), CTRL, 0xf, 0xf, true)); }
constexpr int ROR1 = 0x121, ROR2 = 0x122, ROR4 = 0x124, ROR8 = 0x128, SHL4 = 0x104, SHR4 = 0x114;
__device__ __forceinline__ float tr32(float a, float b) { auto s = __builtin_amdgcn_permlane32_swap(__float_as_uint(a), __float_as_uint(b), false, false); return __uint_as_float(s[0]) + __uint_as_float(s[1]); }
__device__ __forceinline__ float tr16(float a, float b) { auto s = __builtin_amdgcn_permlane16_swap(__float_as_uint(a), __float_as_uint(b), false, false); return __uint_as_float(s[0]) + __uint_as_float(s[1]); }
__device__ __forceinline__ float xmax(float x) {
  auto s = __builtin_amdgcn_permlane16_swap(__float_as_uint(x), __float_as_uint(x), false, false);
  x = fmaxf(__uint_as_float(s[0]), __uint_as_float(s[1]));
  auto t = __builtin_amdgcn_permlane32_swap(__float_as_uint(x), __float_as_uint(x), false, false);
  return fmaxf(__uint_as_float(t[0]), __uint_as_float(t[1]));
}
__device__ __forceinline__ float xsum(float x) { x = tr16(x, x); return tr32(x, x); }
__device__ __forceinline__ float rowsum(float x) { x += dpp<ROR8>(x); x += dpp<ROR4>(x); x += dpp<ROR2>(x); x += dpp<ROR1>(x); return x; }
__device__ __forceinline__ float rowmax(float x) { x = fmaxf(x, dpp<ROR8>(x)); x = fmaxf(x, dpp<ROR4>(x)); x = fmaxf(x, dpp<ROR2>(x)); x = fmaxf(x, dpp<ROR1>(x)); return x; }
__device__ __forceinline__ float dot2(u32 a, u32 b, float c) { return __builtin_amdgcn_fdot2_f32_bf16(__builtin_bit_cast(bf16x2, a), __builtin_bit_cast(bf16x2, b), c, false); }
#define BAR() asm volatile("s_waitcnt lgkmcnt(0)\n\ts_barrier" ::: "memory")
#define WAITV(n) asm volatile("s_waitcnt vmcnt(%0)" :: "n"(n))
#define PIN() __builtin_amdgcn_sched_barrier(0)
#define OPQ(x) asm("" : "+v"(x))
#define FENCE(off) asm volatile("" : "+v"(off) :: "memory")
__device__ __forceinline__ void lds_post(u32* c, int lane) { asm volatile("s_waitcnt lgkmcnt(0)" ::: "memory"); if (lane == 0) __hip_atomic_fetch_add(c, 1u, __ATOMIC_RELAXED, __HIP_MEMORY_SCOPE_WORKGROUP); }
__device__ __forceinline__ u32 lds_post_r(u32* c, int lane) { asm volatile("s_waitcnt lgkmcnt(0)" ::: "memory"); u32 v = 0; if (lane == 0) v = __hip_atomic_fetch_add(c, 1u, __ATOMIC_RELAXED, __HIP_MEMORY_SCOPE_WORKGROUP); v = __builtin_amdgcn_readfirstlane(v); asm volatile("" ::: "memory"); return v; }
__device__ __forceinline__ void lds_wait(u32* c) { u32 spins = 0; while (__builtin_amdgcn_readfirstlane(__hip_atomic_load(c, __ATOMIC_RELAXED, __HIP_MEMORY_SCOPE_WORKGROUP)) < 8u && ++spins < (SPIN_CAP << 6)) {} asm volatile("" ::: "memory"); }

#define BAD2(g) (g[1] != tagA || g[3] != tagB)
#define POLL(BAD, g, off) do { bool bad = BAD(g); u64 bm = __builtin_amdgcn_ballot_w64(bad); if (bm) { u32 spins = 0; \
    do { if (__builtin_popcountll(bm) > SLOWN) __builtin_amdgcn_s_sleep(SLOWS); FENCE(off); if (bad) { g = ld16<SC1>(rws, off, 0); bad = BAD(g); } bm = __builtin_amdgcn_ballot_w64(bad); } while (bm && ++spins < SPIN_CAP); } } while (0)
#define STR2(x) #x
#define STR(x) STR2(x)
#define PLD(r0, r3) "buffer_load_dwordx4 v[" #r0 ":" #r3 "], %[off], %[rs], 0 offen sc1\n\t"
#define PCHK(r0, r1, r2, r3) \
    "v_xor_b32_e32 %[t], %[tA], " r0 "\n\t" \
    "v_bitop3_b32 %[t], %[t], " r1 ", %[tB] bitop3:0xf6\n\t" \
    "v_bitop3_b32 %[t], %[t], " r2 ", %[tC] bitop3:0xf6\n\t" \
    "v_bitop3_b32 %[t], %[t], " r3 ", %[tD] bitop3:0xf6\n\t" \
    "v_and_b32_e32 %[t], 0xfff, %[t]\n\t"
#define PSTEP(r0, r1, r2, r3) \
    "s_waitcnt vmcnt(1)\n\t" \
    PCHK("v" #r0, "v" #r1, "v" #r2, "v" #r3) \
    "v_cmp_eq_u32_e32 vcc, 0, %[t]\n\t" \
    "v_cndmask_b32_e32 %[g0], %[g0], v" #r0 ", vcc\n\t" \
    "v_cndmask_b32_e32 %[g1], %[g1], v" #r1 ", vcc\n\t" \
    "v_cndmask_b32_e32 %[g2], %[g2], v" #r2 ", vcc\n\t" \
    "v_cndmask_b32_e32 %[g3], %[g3], v" #r3 ", vcc\n\t" \
    "s_andn2_b64 exec, exec, vcc\n\t" \
    "s_cbranch_scc0 .Lfin%=\n\t" \
    "s_bcnt1_i32_b64 %[n], exec\n\t" \
    "s_cmp_gt_u32 %[n], " STR(SLOWN) "\n\t" \
    "s_cbranch_scc0 .Ls" #r0 "_%=\n\t" \
    "s_sleep " STR(SLOWS) "\n" \
    ".Ls" #r0 "_%=:\n\t" PLD(r0, r3)
#define POLLP(g, off_) do { u64 sv_; u32 n_, c_, t_; u32 g0_ = g[0], g1_ = g[1], g2_ = g[2], g3_ = g[3]; \
  asm volatile("s_mov_b32 %[c], 0\n\t" \
    PCHK("%[g0]", "%[g1]", "%[g2]", "%[g3]") \
    "v_cmp_ne_u32_e32 vcc, 0, %[t]\n\t" \
    "s_and_b64 vcc, vcc, exec\n\t" \
    "s_cbranch_scc0 .Ldone%=\n\t" \
    "s_mov_b64 %[sv], exec\n\t" \
    "s_mov_b64 exec, vcc\n\t" \
    PLD(100, 103) "s_sleep " STR(STAG) "\n\t" PLD(104, 107) \
    ".Lloop%=:\n\t" \
    PSTEP(100, 101, 102, 103) PSTEP(104, 105, 106, 107) \
    "s_add_u32 %[c], %[c], 1\n\t" \
    "s_cmp_lt_u32 %[c], 0x8000\n\t" \
    "s_cbranch_scc1 .Lloop%=\n" \
    ".Lfin%=:\n\t" \
    "s_add_u32 %[c], %[c], 1\n\t" \
    "s_mov_b64 exec, %[sv]\n" \
    ".Ldone%=:" \
    : [g0] "+v"(g0_), [g1] "+v"(g1_), [g2] "+v"(g2_), [g3] "+v"(g3_), "+{v[100:103]}"(pt0), "+{v[104:107]}"(pt1), "+{v[108:111]}"(pt2), [t] "=&v"(t_), [sv] "=&s"(sv_), [n] "=&s"(n_), [c] "=&s"(c_) \
    : [off] "v"(off_), [rs] "s"(rws), [tA] "s"(tagA), [tB] "s"(tagB), [tC] "s"(tagC), [tD] "s"(tagD) : "vcc", "scc", "memory"); \
  g[0] = g0_; g[1] = g1_; g[2] = g2_; g[3] = g3_; } while (0)

__global__ void __launch_bounds__(512, 2) fused_decode(const u16* xa, const u16* ka, const u16* va, const u16* wqa, const u16* woa,
                                                       const u16* xb, const u16* kb, const u16* vb, const u16* wqb, const u16* wob,
                                                       u16* out, unsigned char* ws) {
  __shared__ __attribute__((aligned(16))) float lds[L_END];
  u32* ldsu = reinterpret_cast<u32*>(lds);
  const int tid = threadIdx.x, lane = tid & 63, wave = __builtin_amdgcn_readfirstlane(tid >> 6), wg = blockIdx.x;
  const int hb = wg >> 7, loc = wg & 127;
  const int bh = wg >> 4, sp = wg & 15, b = bh >> 3, kvh = bh & 7;
  ldsu[L_CN + (tid & 3)] = 0u;
  int cs = (loc & ~15) | ((loc & 7) << 1) | ((loc >> 3) & 1);
  u32 epo = tid == 0 ? 0u : OOB, xo = 128 * wave + 8 * (lane >> 2), vo = (lane >> 2) * 32768 + (lane & 3) * 16, wqo = wave * 524288 + cs * 64;
  const u32 bsa = dep<'x', 3, true>(wave) | dep<'0', 4, true>(sp), bsb = dep<'x', 3, false>(wave) | dep<'0', 4, false>(sp);
  const u32 kin = (lane >> 4) * 64 + (lane & 3) * 16, vin = (lane & 15) * 16;
  u32 kvoa = bsa | dep<'p', 2, true>(lane >> 2) | kin, kvob = bsb | dep<'p', 2, false>(lane >> 2) | kin;
  const u32 vvoa = bsa | dep<'p', 2, true>(lane >> 4) | vin, vvob = bsb | dep<'p', 2, false>(lane >> 4) | vin;
  const u32 rot = sp + 2 * bh;
  u32 so[20], bho = bh * (SK * 256);
#pragma unroll
  for (int i = 0; i < NSO; ++i) { so[i] = i < 4 ? dep<'a', 2, true>(rot + i) : dep<'a', 4, false>(rot + i - 4); asm volatile("" : "+s"(so[i])); }
  asm volatile("" : "+v"(epo), "+v"(xo), "+v"(vo), "+v"(kvoa), "+v"(kvob));
  asm volatile("" : "+s"(cs)); asm volatile("" : "+s"(wqo)); asm volatile("" : "+s"(bho));
  PIN();
  asm volatile("" :: "s"(xa), "s"(ka), "s"(va), "s"(wqa), "s"(woa), "s"(xb), "s"(kb), "s"(vb), "s"(wqb), "s"(wob), "s"(out), "s"(ws) : "memory");
  PIN();
  const rsrc_t rws = mk(ws - ((uintptr_t)ws & ((2u << 20) - 1)), WS_END);
  int vote = 0; u32 half = 0;
  if (wg < 8) {
    const u32 cl = (wave + (wave >> 2) + wg) & 1;
    half = (__builtin_amdgcn_s_getreg(20 | (3 << 11)) >> 2) & 1;
    const u64 t0 = __builtin_amdgcn_s_memtime();
    asm volatile("s_waitcnt lgkmcnt(0)" ::: "memory");
    const u32 pa = WS_PR + cl * 8192 + wg * 16384 + (wave >> 1) * 1024 + (wave & 1) * 4096 + (wg & 3) * 256;
    if (lane == 0) __builtin_amdgcn_raw_buffer_store_b32(0u, rws, 0, pa, SC1);
    asm volatile("s_waitcnt vmcnt(0)" ::: "memory");
    const int dt = min((int)(__builtin_amdgcn_s_memtime() - t0), 1200);
    vote = (cl ^ half) ? -dt : dt;
  }
  u32x4 ep = ld16<SC1>(rws, epo, 0);

  const rsrc_t rx = mk(hb ? xb : xa, 2048);
  const u32x2 x0 = ld8<0>(rx, xo, 0), x1 = ld8<0>(rx, xo + 1024, 0);
  u32x4 w[4];
  { const rsrc_t r = mk((const char*)(hb ? wqb : wqa) + wqo, 63 * 8192 + 64);
#pragma unroll
    for (int t = 0; t < 4; ++t) w[t] = ld16<NT>(r, vo, t * 8192); }
  PIN();
  const rsrc_t rka = mk((const char*)ka + (bho + (SK - WIN) * 256), WIN * 256);
  const rsrc_t rkb = mk((const char*)kb + bho, SK * 256);
  const rsrc_t rva = mk((const char*)va + (bho + (SK - WIN) * 256), WIN * 256);
  const rsrc_t rvb = mk((const char*)vb + bho, SK * 256);
  u32x4 K[20], V[20];
#define LDK(i) do { if ((i) >= NSO) so[i] = (i) < 4 ? dep<'a', 2, true>(rot + (i)) : dep<'a', 4, false>(rot + (i) - 4); K[i] = ((i) < 4 ? ld16<NT>(rka, kvoa, so[i]) : ld16<NT>(rkb, kvob, so[i])); } while (0)
#define LDV(i) V[i] = ((i) < 4 ? ld16<NT>(rva, vvoa, so[i]) : ld16<NT>(rvb, vvob, so[i]))
#define SLOT2(j) do { if ((j) < 20) { PIN(); WAITV(DEP); LDV((j) < 20 ? (j) : 0); PIN(); } } while (0)
#define SLOT(u) do { if ((u) < 20) { PIN(); WAITV(DEP); LDK((u) < 20 ? (u) : 0); PIN(); } else SLOT2((u) - 20); } while (0)
#pragma unroll
  for (int i = 0; i < NPRE; ++i) LDK(i);
  PIN();
  BAR();
  PIN();
  {
    float a[2][8];
#pragma unroll
    for (int e = 0; e < 8; ++e) { a[0][e] = 0.f; a[1][e] = 0.f; }
#pragma unroll
    for (int t = 0; t < 4; ++t) {
      PIN(); WAITV(NPRE + 3); LDK(NPRE + t); PIN();
      const float f0 = (t & 1) ? bhi(x0[t >> 1]) : blo(x0[t >> 1]), f1 = (t & 1) ? bhi(x1[t >> 1]) : blo(x1[t >> 1]);
#pragma unroll
      for (int e = 0; e < 4; ++e) { const float l = blo(w[t][e]), h = bhi(w[t][e]);
        a[0][2 * e] = fmaf(f0, l, a[0][2 * e]); a[0][2 * e + 1] = fmaf(f0, h, a[0][2 * e + 1]);
        a[1][2 * e] = fmaf(f1, l, a[1][2 * e]); a[1][2 * e + 1] = fmaf(f1, h, a[1][2 * e + 1]); } }
    float y[8], z[4], u[2];
#pragma unroll
    for (int e = 0; e < 8; ++e) y[e] = tr32(a[0][e], a[1][e]);
#pragma unroll
    for (int e = 0; e < 4; ++e) z[e] = tr16(y[e], y[e + 4]);
#pragma unroll
    for (int e = 0; e < 2; ++e) { const float t1 = z[e] + dpp<ROR8>(z[e]), t2 = z[e + 2] + dpp<ROR8>(z[e + 2]); u[e] = (lane & 8) ? t2 : t1; }
    const float t1 = u[0] + dpp<SHL4>(u[0]), t2 = u[1] + dpp<SHR4>(u[1]);
    const float qv = (lane & 4) ? t2 : t1;
    lds[L_QP + wave * 64 + (lane & 32) + 8 * (lane & 3) + ((lane >> 2) & 4) + ((lane >> 2) & 2) + ((lane >> 2) & 1)] = qv;
    ep += u32x4{0x9e3779b1u, 0x85ebca77u, 0xc2b2ae3du, 0x27d4eb2fu};
    if (tid == 0) *(u32x4*)&ldsu[L_TG] = u32x4{ep[0], __builtin_rotateright32(ep[1], 12), ep[2], __builtin_rotateright32(ep[3], 12)};
  }
  if (wg < 8 && lane == 0) __hip_atomic_fetch_add(&ldsu[L_CN + 3], (u32)vote, __ATOMIC_RELAXED, __HIP_MEMORY_SCOPE_WORKGROUP);
  if (lds_post_r(&ldsu[L_CN + 2], lane) == 7u) {
    const int l16 = lane & 15;
    const u32x2 tg = *(const u32x2*)&ldsu[L_TG];
    f32x4 s = *(const f32x4*)&lds[L_QP + 4 * l16];
#pragma unroll
    for (int ww = 1; ww < 8; ++ww) s += *(const f32x4*)&lds[L_QP + ww * 64 + 4 * l16];
    const u32x4 g = {pk(s[0], s[1]), tg[0], pk(s[2], s[3]), tg[1]};
    if (lane < 16) st16(rws, WS_Q + ((hb * 2 + (l16 >> 3)) * 1024 + cs * 8 + (l16 & 7)) * 16, 0, g);
    if (wg < 8 && lane == 0) st16(rws, WS_VT + wg * 16, 0, u32x4{__float_as_uint((float)(int)ldsu[L_CN + 3]), tg[0], half, tg[1]});
  }
  PIN();
  const int qi = wave * 32 + (lane & 31);
  u32 qoff = lane < 32 ? WS_Q + (((qi >> 7) * 2 + b) * 1024 + kvh * 128 + (qi & 127)) * 16 : lane < 40 ? WS_VT + (lane - 32) * 16 : OOB;
  u32x4 qg;
#pragma unroll
  for (int u = NPRE + 4; u <= QC; ++u) { SLOT(u); if (u == QP) { qg = ld16<SC1>(rws, qoff, 0); PIN(); } }
  lds_wait(&ldsu[L_CN + 2]);
  const u32x4 tg = *(const u32x4*)&ldsu[L_TG];
  const u32 tagA = __builtin_amdgcn_readfirstlane(tg[0]), tagB = __builtin_amdgcn_readfirstlane(tg[1]);
  const u32 tagC = __builtin_amdgcn_readfirstlane(tg[2]), tagD = __builtin_amdgcn_readfirstlane(tg[3]);
#define BADQ(g) (lane < 40 && BAD2(g))
  POLL(BADQ, qg, qoff);
  if (lane < 32) *(u32x2*)&ldsu[L_QS + 2 * qi] = u32x2{qg[0], qg[2]};
  lds_post(&ldsu[L_CN], lane);
  const u32 hm = ((u32)(__builtin_amdgcn_ballot_w64((rowsum(__uint_as_float(qg[0])) > 0.f) != (qg[2] != 0u)) >> 32)) ^ 255u;
#pragma unroll
  for (int u = QC + 1; u <= QW; ++u) SLOT(u);
  lds_wait(&ldsu[L_CN]);
  float s[20];
  {
    u32x4 qf[2];
#pragma unroll
    for (int br = 0; br < 2; ++br) qf[br] = *(const u32x4*)&ldsu[L_QS + br * 256 + ((lane >> 2) & 3) * 64 + (lane >> 4) * 16 + (lane & 3) * 4];
#pragma unroll
    for (int t = 0; t < 5; ++t) {
      float c[4]; f32x4 d[4];
#pragma unroll
      for (int r = 0; r < 4; ++r) d[r] = __builtin_amdgcn_mfma_f32_16x16x32_bf16(__builtin_bit_cast(bf16x8, K[4 * t + r]), __builtin_bit_cast(bf16x8, qf[t ? 1 : 0]), f32x4{0.f, 0.f, 0.f, 0.f}, 0, 0, 0);
      PIN();
#pragma unroll
      for (int r = 0; r < 4; ++r) { c[r] = dpp<0x00>(d[r][0]); OPQ(c[r]); }
#pragma unroll
      for (int r = 0; r < 4; ++r) { c[r] += dpp<0x55>(d[r][1]); OPQ(c[r]); }
#pragma unroll
      for (int r = 0; r < 4; ++r) { c[r] += dpp<0xaa>(d[r][2]); OPQ(c[r]); }
#pragma unroll
      for (int r = 0; r < 4; ++r) { c[r] += dpp<0xff>(d[r][3]); OPQ(c[r]); }
#pragma unroll
      for (int r = 0; r < 4; r += 2) { const u32 p1 = pk(c[r], c[r + 1]); const u32 p2 = pk(blo(p1) * SCALE, bhi(p1) * SCALE); s[4 * t + r] = blo(p2); s[4 * t + r + 1] = bhi(p2); }
      SLOT(QW + 1 + t);
    }
  }
  float m2[2];
  {
    float ma = fmaxf(fmaxf(s[0], s[1]), fmaxf(s[2], s[3])), mb = -INFINITY;
#pragma unroll
    for (int t = 1; t < 5; ++t) mb = fmaxf(mb, fmaxf(fmaxf(s[4 * t], s[4 * t + 1]), fmaxf(s[4 * t + 2], s[4 * t + 3])));
    m2[0] = fmaxf(xmax(ma) * L2E, -3.0e38f); m2[1] = fmaxf(xmax(mb) * L2E, -3.0e38f);
  }
  u32 P[20];
  {
    float la = 0.f, lb = 0.f;
#pragma unroll
    for (int i = 0; i < 20; ++i) { const float p = __builtin_amdgcn_exp2f(fmaf(s[i], L2E, -m2[i >= 4])); if (i >= 4) lb += p; else la += p; P[i] = pk(p, p); if (i == 7) SLOT(QW + 6); }
    la = xsum(la); lb = xsum(lb);
    if ((lane & 0x33) == 0) { lds[L_MX + (lane >> 2) * 8 + wave] = m2[0]; lds[L_MX + 32 + (lane >> 2) * 8 + wave] = m2[1];
                              lds[L_LS + (lane >> 2) * 8 + wave] = la; lds[L_LS + 32 + (lane >> 2) * 8 + wave] = lb; }
    if (lds_post_r(&ldsu[L_CN + 1], lane) == 7u) {
      const int i8 = lane & 7;
      const f32x4 a = *(const f32x4*)&lds[L_MX + i8 * 8], c = *(const f32x4*)&lds[L_MX + i8 * 8 + 4];
      const f32x4 d = *(const f32x4*)&lds[L_LS + i8 * 8], e = *(const f32x4*)&lds[L_LS + i8 * 8 + 4];
      const float mm = fmaxf(fmaxf(fmaxf(a[0], a[1]), fmaxf(a[2], a[3])), fmaxf(fmaxf(c[0], c[1]), fmaxf(c[2], c[3])));
      float ll = 0.f;
#pragma unroll
      for (int j = 0; j < 4; ++j) ll += d[j] * __builtin_amdgcn_exp2f(a[j] - mm) + e[j] * __builtin_amdgcn_exp2f(c[j] - mm);
      const u32x4 g = {__float_as_uint(mm), tagA, __float_as_uint(ll), tagB};
      if (lane < 8) st16(rws, WS_ML + ((bh * 8 + i8) * 16 + sp) * 16, 0, g);
    }
  }
  PIN();
  const int ep4 = lane & 3;
  const u32 mk0 = ep4 == 0 ? 0x0000ffffu : ep4 == 1 ? 0xffff0000u : 0u, mk1 = ep4 == 2 ? 0x0000ffffu : ep4 == 3 ? 0xffff0000u : 0u;
  f32x4 o[2][2];
  float fb1 = 0.f, wgt = 0.f;
  u32 moff = lane < 16 ? WS_ML + ((bh * 8 + wave) * 16 + lane) * 16 : OOB;
  u32x4 mg;
  u32 osv = WS_O + lane * 8 + ((hm >> ((wave & 1) * 4 + (lane >> 4))) & 1) * 8192, oss = (((wave >> 2) * 2 + b) * 32 + kvh * 4 + (wave & 3)) * 16384 + sp * 512;
  u32 psv = lane < 32 ? WS_P + (lane >> 4) * 2048 + (lane & 15) * 16 + (((hm ^ 255u) >> ((wave & 1) * 4 + ((lane >> 2) & 3))) & 1) * 8192 : OOB, pss = (wg >> 1) * 16384 + (wg & 1) * 4096 + wave * 256;
  u32 ot0 = (lane & 1) ? tagC : tagA, ot1 = (lane & 1) ? tagD : tagB;
  u32 rda = (L_RED + (wave >> 2) * RED_B + ((lane >> 1) & 1) * RED_U + (16 * (wave & 3) + (lane >> 2)) * 4 + 2 * (lane & 1)) * 4;
  u32 oba = L_OB * 4 + ((wave & 3) * 16 + 4 * (lane & 3) + (lane >> 4)) * 2, ova = (L_OB + ((lane >> 2) & 1) * 16 + (lane >> 4) * 4) * 4;
  u32 fsa = (L_FS + (4 * (lane & 3) + (lane >> 4)) * 8 + wave) * 4, fra = (L_FS + (lane & 31) * 4) * 4;
  u32 outv = (lane & 0x23) == 0 ? (loc >> 5) * 2048 + hb * 1024 + (loc & 31) * 32 + lane : OOB;
  const u16* wop = (hb ? wob : woa) + (size_t)32 * loc * 512 + 64 * wave;
  u32 wvo = (lane >> 4) * 8192 + (lane & 15) * 8;
  char* const ldc = reinterpret_cast<char*>(lds);
  u32 ooff = WS_O + ((hb * 2 + ((wave >> 1) & 1)) * 32 + (loc >> 2)) * 16384 + ((hm >> (loc & 7)) & 1) * 8192 + (lane >> 2) * 512 + (32 * (loc & 3) + 16 * (wave & 1) + 4 * (lane & 3)) * 4;
  u32 poff = WS_P + (hb * 64 + wave * 8 + (lane >> 3)) * 16384 + ((lane >> 2) & 1) * 4096 + (((hm ^ 255u) >> (loc & 7)) & 1) * 8192 + loc * 64 + (lane & 3) * 16;
#pragma unroll
  for (int br = 0; br < 2; ++br) { o[br][0] = f32x4{0.f, 0.f, 0.f, 0.f}; o[br][1] = f32x4{0.f, 0.f, 0.f, 0.f}; }
#pragma unroll
  for (int i = 0; i < 20; ++i) {
    if (i + DEP >= QW + 7 - 20) SLOT2(i + DEP);
    if (i + DEP == MP) { mg = ld16<SC1>(rws, moff, 0); PIN(); }
    if (i == HOIST) { asm volatile("" : "+v"(osv), "+v"(psv), "+v"(ot0), "+v"(ot1), "+v"(rda), "+v"(oba), "+v"(ova), "+v"(fsa), "+v"(fra), "+v"(outv), "+v"(wvo), "+v"(ooff), "+v"(poff));
      asm volatile("" : "+s"(oss)); asm volatile("" : "+s"(pss)); asm volatile("" : "+s"(wop)); PIN(); }
    const int br = i >= 4;
    const s16x4 a = __builtin_bit_cast(s16x4, u32x2{P[i] & mk0, P[i] & mk1});
    o[br][0] = __builtin_amdgcn_mfma_f32_16x16x16bf16_1k(a, __builtin_bit_cast(s16x4, u32x2{V[i][0], V[i][1]}), o[br][0], 0, 0, 0);
    o[br][1] = __builtin_amdgcn_mfma_f32_16x16x16bf16_1k(a, __builtin_bit_cast(s16x4, u32x2{V[i][2], V[i][3]}), o[br][1], 0, 0, 0);
    if (i == ISYNC) {
      PIN();
      lds_wait(&ldsu[L_CN + 1]);
      float f[2];
#pragma unroll
      for (int br = 0; br < 2; ++br) {
        const int base = L_MX + br * 32 + (lane >> 4) * 8;
        const f32x4 a = *(const f32x4*)&lds[base], c = *(const f32x4*)&lds[base + 4];
        f[br] = __builtin_amdgcn_exp2f(lds[base + wave] - fmaxf(fmaxf(fmaxf(a[0], a[1]), fmaxf(a[2], a[3])), fmaxf(fmaxf(c[0], c[1]), fmaxf(c[2], c[3])))); }
      fb1 = f[1];
      *(f32x4*)&lds[L_RED + wave * RED_W + 4 * lane] = o[0][0] * f[0]; *(f32x4*)&lds[L_RED + wave * RED_W + RED_U + 4 * lane] = o[0][1] * f[0];
      PIN();
    }
    if (i == MP) {
      PIN();
#define BADM(g) (lane < 16 && BAD2(g))
      POLL(BADM, mg, moff);
      const float ms = __uint_as_float(mg[0]), M = rowmax(ms), ex = __builtin_amdgcn_exp2f(ms - M);
      wgt = __builtin_bit_cast(float, __builtin_amdgcn_readlane(__builtin_bit_cast(int, ex / rowsum(ex * __uint_as_float(mg[2]))), sp));
      PIN();
    }
  }
  o[1][0] *= fb1; o[1][1] *= fb1;
  *(f32x4*)&lds[L_RED + wave * RED_W + RED_B + 4 * lane] = o[1][0];
  *(f32x4*)&lds[L_RED + wave * RED_W + RED_B + RED_U + 4 * lane] = o[1][1];
  PIN();
  u32x2 wo[8];
  { const rsrc_t r = mk(wop, 31 * 1024 + 128);
#pragma unroll
    for (int j = 0; j < 8; ++j) wo[j] = ld8<NT>(r, wvo + (j & 3) * 1024, (j >> 2) * 4096);
    PIN(); }
  BAR();
  {
    f32x2 t[8];
#pragma unroll
    for (int ww = 0; ww < 8; ++ww) t[ww] = *(const f32x2*)(ldc + rda + ww * RED_W * 4);
    const f32x2 sum = ((t[0] + t[1]) + (t[2] + t[3])) + ((t[4] + t[5]) + (t[6] + t[7]));
    st8(rws, osv, oss, u32x2{tagw(sum[0] * wgt, ot0), tagw(sum[1] * wgt, ot1)});
  }
  PIN();
  u32x4 pt0, pt1, pt2;
  asm volatile("" : "={v[100:103]}"(pt0), "={v[104:107]}"(pt1), "={v[108:111]}"(pt2));
  {
    u32x4 og;
    if (wave >= 4) og = ld16<SC1>(rws, ooff, 0);
    PIN();
    u32x4 wb[4];
#pragma unroll
    for (int t = 0; t < 4; ++t)
#pragma unroll
      for (int d = 0; d < 4; ++d) wb[t][d] = __builtin_amdgcn_perm(wo[2 * d + 1][t >> 1], wo[2 * d][t >> 1], (t & 1) ? 0x07060302u : 0x05040100u);
#pragma unroll
    for (int t = 0; t < 4; ++t) asm volatile("" : "+v"(wb[t]));
    PIN();
    if (wave >= 4) {
      POLLP(og, ooff);
      float z = tr16(tr32(untag(og[0]), untag(og[2])), tr32(untag(og[1]), untag(og[3])));
      z += dpp<ROR4>(z); z += dpp<ROR8>(z);
      if ((lane & 12) == 0) *(u16*)(ldc + oba) = (u16)pk(z, z);
    }
    BAR();
    const u32x4 ov = *(const u32x4*)(ldc + ova);
    f32x4 d[4];
#pragma unroll
    for (int t = 0; t < 4; ++t) d[t] = __builtin_amdgcn_mfma_f32_16x16x32_bf16(__builtin_bit_cast(bf16x8, ov), __builtin_bit_cast(bf16x8, wb[t]), f32x4{0.f, 0.f, 0.f, 0.f}, 0, 0, 0);
    st16(rws, psv, pss, u32x4{tagw(d[0][0], tagA), tagw(d[1][0], tagB), tagw(d[2][0], tagC), tagw(d[3][0], tagD)});
  }
  PIN();
  if (loc < 64) {
    __builtin_amdgcn_s_sleep(10);
    u32x4 g = ld16<SC1>(rws, poff, 0);
    POLLP(g, poff);
    float z = tr16(tr32(untag(g[0]), untag(g[2])), tr32(untag(g[1]), untag(g[3])));
    z += dpp<ROR4>(z); z += dpp<ROR8>(z);
    if ((lane & 12) == 0) *(float*)(ldc + fsa) = z;
    BAR();
    if (wave == 0) {
      const f32x4 c = *(const f32x4*)(ldc + fra);
      float r = (c[0] + c[1]) + (c[2] + c[3]);
      r += dpp<0xb1>(r);
      const float r2 = dpp<0x4e>(r);
      __builtin_amdgcn_raw_buffer_store_b32(pk(r, r2), mk(out, 4096), outv, 0, 0);
      if (wg == 0 && lane == 0) st16(rws, 0, 0, u32x4{tagA, __builtin_rotateleft32(tagB, 12), tagC, __builtin_rotateleft32(tagD, 12)});
    }
  }
  asm volatile("s_waitcnt vmcnt(0)" :: "{v[100:103]}"(pt0), "{v[104:107]}"(pt1), "{v[108:111]}"(pt2));
}

extern "C" void kernel_launch(void* const* d_in, const int* in_sizes, int n_in, void* d_out, int out_size, void* d_ws, size_t ws_size, hipStream_t stream) {
  fused_decode<<<256, 512, 0, stream>>>((const u16*)d_in[0], (const u16*)d_in[1], (const u16*)d_in[2], (const u16*)d_in[3], (const u16*)d_in[4],
                                        (const u16*)d_in[5], (const u16*)d_in[6], (const u16*)d_in[7], (const u16*)d_in[8], (const u16*)d_in[9],
                                        (u16*)d_out, (unsigned char*)d_ws + ws_size - WS_END);
}
```
